```python
import torch

def _softmax(x, axis=-1):
    return torch.softmax(x, dim=axis)
def _gelu(x):
    return torch.nn.functional.gelu(x, approximate="tanh")
def _silu(x):
    return torch.nn.functional.silu(x)
def _sigmoid(x):
    return torch.sigmoid(x)
def _layernorm(x, axis=-1, eps=1e-5):
    m = x.mean(dim=axis, keepdim=True)
    v = x.var(dim=axis, keepdim=True, unbiased=False)
    return (x - m) / torch.sqrt(v + eps)
def _scatter_add(tgt, idx, src, axis=0):
    return tgt.clone().index_add_(axis, idx, src)
def _segment_sum(src, seg, n):
    out = torch.zeros((n,) + src.shape[1:], dtype=src.dtype)
    return out.index_add_(0, seg, src)
def _pad(x, pw):
    flat = tuple(v for pair in reversed(pw) for v in pair)
    return torch.nn.functional.pad(x, flat)

kernel_name = 'standalone__detection_seg_dino__vision'

def setup_inputs():

    _w = (lambda a: (lambda t: t.to(torch.bfloat16) if t.is_floating_point() else t)(torch.as_tensor(a)))
    H = W = (224 // 2) * 2
    C = 192
    Nq = 16
    sc = C ** -0.5
    return {
        "q": _w((lambda *s: torch.randn(*s, dtype=torch.bfloat16) * 0.05)(1, Nq, C)),
        "f0": _w((lambda *s: torch.randn(*s, dtype=torch.bfloat16) * 0.8)(1, H, W, C)),
        "f1": _w((lambda *s: torch.randn(*s, dtype=torch.bfloat16) * 0.8)(1, H // 2, W // 2, C)),
        "wq": _w((lambda *s: torch.randn(*s, dtype=torch.bfloat16) * 1.6)(C, C) * sc),
        "wk": _w((lambda *s: torch.randn(*s, dtype=torch.bfloat16) * 1.6)(C, C) * sc),
        "wv": _w((lambda *s: torch.randn(*s, dtype=torch.bfloat16) * 0.8)(C, C) * sc),
        "wo": _w((lambda *s: torch.randn(*s, dtype=torch.bfloat16) * 0.8)(C, C) * sc),
    }

def reference(q, f0, f1, wq, wk, wv, wo):
    B, H, W, C = f0.shape
    f1u = (lambda x, n, axis: torch.repeat_interleave(x, n, dim=axis))((lambda x, n, axis: torch.repeat_interleave(x, n, dim=axis))(f1, 2, axis=1), 2, axis=2)
    fused = f0 + f1u
    feats = _layernorm(fused.reshape(B, H * W, C), axis=-1)
    qn = _layernorm(q, axis=-1)
    Q = torch.einsum("bnc,cd->bnd", qn, wq)
    K = torch.einsum("bmc,cd->bmd", feats, wk)
    V = torch.einsum("bmc,cd->bmd", feats, wv)
    scale = C ** -0.5
    logits = torch.einsum("bnd,bmd->bnm", Q, K) * scale
    attn = _softmax(logits, axis=-1)
    ctx = torch.einsum("bnm,bmd->bnd", attn, V)
    return q + torch.einsum("bnd,dc->bnc", ctx, wo)
```

```cpp
#include <hip/hip_runtime.h>
#include <hip/hip_bf16.h>
#include <stdint.h>

typedef unsigned short u16;
typedef unsigned long long u64;
typedef short s16x4 __attribute__((ext_vector_type(4)));
typedef short s16x2 __attribute__((ext_vector_type(2)));
typedef unsigned short u16x2 __attribute__((ext_vector_type(2)));
typedef float f32x4 __attribute__((ext_vector_type(4)));
typedef float f32x2 __attribute__((ext_vector_type(2)));
typedef unsigned u32x16 __attribute__((ext_vector_type(16)));
typedef unsigned u32x4 __attribute__((ext_vector_type(4)));
typedef unsigned u32x2 __attribute__((ext_vector_type(2)));
typedef __bf16 bf16x2_t __attribute__((ext_vector_type(2)));
typedef __bf16 bf16x8_t __attribute__((ext_vector_type(8)));
typedef _Float16 f16x2_t __attribute__((ext_vector_type(2)));
#define LAS __attribute__((address_space(3)))
#define GAS __attribute__((address_space(1)))
#define DI __device__ __forceinline__
#define RLX __ATOMIC_RELAXED, __HIP_MEMORY_SCOPE_AGENT

#define STAMPS 0
#define WIN 12
#define CODE_KB 19
#define PRO_SIG 4
#define WNT ""
#define PNT ""
#define FORCE_SLOW 0

constexpr int C = 192;
constexpr int NPRO = 12, NPROD = 196, NRED = 32, NBLK = NPRO + NPROD + NRED, NTHR = 704;
constexpr float EPS = 1e-5f;
constexpr unsigned SENT = 0xFFFFFFFFu;
constexpr int REC = 208, SLABR = 40960;
constexpr int WS_EPOCH = 0, WS_RDY = 1024, WS_QF = 4096, WS_QK = 8192, WS_CNT = 32768, WS_SLAB = 131072;
constexpr int WS_BYTES = WS_SLAB + 32 * SLABR;
constexpr size_t WS_STAMP = 2u << 20;
#define WS_OFF(sz) (((sz) - (4u << 20)) & ~(size_t)4095)
constexpr int UB = 15360;
constexpr int L_P = 8 * UB;
constexpr int O_QK = 12288;
constexpr int L_SM = L_P + O_QK + 6144;
constexpr int L_PF = L_SM + 1024;
constexpr int LDS_BYTES = L_PF + 256;
constexpr int SPIN = 1 << 12;

DI float bf_lo(unsigned u) { return __uint_as_float(u << 16); }
DI unsigned cvtpk(float lo, float hi) { bf16x2_t r; r[0] = (__bf16)lo; r[1] = (__bf16)hi; return __builtin_bit_cast(unsigned, r); }
DI unsigned cvtpk_h(float lo, float hi) { f16x2_t r; r[0] = (_Float16)lo; r[1] = (_Float16)hi; return __builtin_bit_cast(unsigned, r); }
DI float dot2(unsigned a, unsigned b, float c) { return __builtin_amdgcn_fdot2_f32_bf16(__builtin_bit_cast(bf16x2_t, a), __builtin_bit_cast(bf16x2_t, b), c, false); }
DI float fmx(float a, float b) { return __builtin_elementwise_maximum(a, b); }
DI float fmx4(f32x4 a) { return fmx(fmx(a[0], a[1]), fmx(a[2], a[3])); }
template <int CTRL> DI float dpp(float x) { return __builtin_bit_cast(float, __builtin_amdgcn_mov_dpp(__builtin_bit_cast(int, x), CTRL, 0xf, 0xf, true)); }
DI float xrow_max(float x) {
  auto s = __builtin_amdgcn_permlane16_swap(__float_as_uint(x), __float_as_uint(x), false, false);
  x = fmx(__uint_as_float(s[0]), __uint_as_float(s[1]));
  auto t = __builtin_amdgcn_permlane32_swap(__float_as_uint(x), __float_as_uint(x), false, false);
  return fmx(__uint_as_float(t[0]), __uint_as_float(t[1]));
}
DI float fold32(float a, float b) { auto t = __builtin_amdgcn_permlane32_swap(__float_as_uint(a), __float_as_uint(b), false, false); return __uint_as_float(t[0]) + __uint_as_float(t[1]); }
DI float fold16(float a, float b) { auto t = __builtin_amdgcn_permlane16_swap(__float_as_uint(a), __float_as_uint(b), false, false); return __uint_as_float(t[0]) + __uint_as_float(t[1]); }
DI float xrow_sum(float x) { x = fold16(x, x); return fold32(x, x); }
DI float row16_max(float x) { x = fmx(x, dpp<0xB1>(x)); x = fmx(x, dpp<0x4E>(x)); x = fmx(x, dpp<0x124>(x)); x = fmx(x, dpp<0x128>(x)); return x; }
DI float row16_sum(float x) { x += dpp<0xB1>(x); x += dpp<0x4E>(x); x += dpp<0x124>(x); x += dpp<0x128>(x); return x; }
DI f32x4 MFMA(u32x4 a, u32x4 b, f32x4 c) { return __builtin_amdgcn_mfma_f32_16x16x32_bf16(__builtin_bit_cast(bf16x8_t, a), __builtin_bit_cast(bf16x8_t, b), c, 0, 0, 0); }
DI u64 rt() { return __builtin_amdgcn_s_memrealtime(); }
template <int N> DI void waitvm() { asm volatile("s_waitcnt vmcnt(%0)" ::"i"(N) : "memory"); }
#define SB() __builtin_amdgcn_sched_barrier(0)
DI void bar() { SB(); asm volatile("s_waitcnt lgkmcnt(0)\n\ts_barrier" ::: "memory"); SB(); }
#define PIN(x) asm volatile("" : "+v"(x))
#define PINS(x) asm volatile("" : "+s"(x))
typedef LAS char* lp;
DI lp LP(unsigned a) { return (lp)(size_t)a; }
DI u32x4 R128(unsigned base, int off) { return *(LAS const u32x4*)(LP(base) + off); }
DI f32x4 R128F(unsigned base, int off) { return *(LAS const f32x4*)(LP(base) + off); }
DI u32x2 R64(unsigned base, int off) { return *(LAS const u32x2*)(LP(base) + off); }
DI float R32F(unsigned base, int off) { return *(LAS const float*)(LP(base) + off); }
DI void W128(unsigned base, int off, u32x4 v) { *(LAS u32x4*)(LP(base) + off) = v; }
DI void W64(unsigned base, int off, u32x2 v) { *(LAS u32x2*)(LP(base) + off) = v; }
DI void W32(unsigned base, int off, unsigned v) { *(LAS unsigned*)(LP(base) + off) = v; }
DI u32x4 TR2(unsigned ba, int offa, unsigned bb, int offb) {
  s16x4 a = __builtin_amdgcn_ds_read_tr16_b64_v4i16((LAS s16x4*)(LP(ba) + offa));
  s16x4 b = __builtin_amdgcn_ds_read_tr16_b64_v4i16((LAS s16x4*)(LP(bb) + offb));
  return __builtin_bit_cast(u32x4, __builtin_shufflevector(a, b, 0, 1, 2, 3, 4, 5, 6, 7));
}
template <int LOFF, int SOFF> DI void dmai(u32x4 rsrc, unsigned voff, unsigned sbase, unsigned lbase) {
  unsigned t;
  asm volatile("s_add_u32 m0, %4, %5\n\ts_add_u32 %0, %3, %6\n\tbuffer_load_dwordx4 %1, %2, %0 offen lds" : "=&s"(t) : "v"(voff), "s"(rsrc), "s"(sbase), "s"(lbase), "i"(LOFF), "i"(SOFF) : "memory", "scc");
}
DI void dma_loop(u32x4 rsrc, unsigned voff, unsigned so, unsigned ss, unsigned lb, unsigned ls, unsigned n) {
  asm volatile("1:\n\ts_mov_b32 m0, %1\n\ts_add_u32 %1, %1, %6\n\tbuffer_load_dwordx4 %3, %4, %0 offen " WNT " lds\n\ts_add_u32 %0, %0, %5\n\ts_sub_u32 %2, %2, 1\n\ts_cmp_lg_u32 %2, 0\n\ts_cbranch_scc1 1b"
               : "+s"(so), "+s"(lb), "+s"(n) : "v"(voff), "s"(rsrc), "s"(ss), "s"(ls) : "memory", "scc");
}
DI void dma4(u32x4 rsrc, unsigned voff) {
  asm volatile("s_mov_b32 m0, %2\n\ts_nop 0\n\tbuffer_load_dword %0, %1, 0 offen " PNT " lds" ::"v"(voff), "s"(rsrc), "i"(L_PF) : "memory");
}
DI u32x4 mkrs(unsigned lo, unsigned hi, unsigned bytes) { u32x4 r = {lo, hi & 0xffffu, bytes, 0x00020000u}; return r; }
template <unsigned MASK, int SLEEP> DI unsigned poll_atomic(GAS unsigned* p, unsigned want) {
  unsigned v = ~want, z = 0, n = 0; u64 sav;
  asm volatile("" : "+v"(z));
  asm volatile(
      "s_mov_b64 %2, exec\n\ts_mov_b64 exec, %6\n"
      "1:\n\t"
      "global_atomic_add %0, %4, %3, off sc0\n\t"
      "s_waitcnt vmcnt(3)\n\t"
      "v_cmp_ne_u32 vcc, %5, %0\n\t"
      "s_add_u32 %1, %1, 1\n\t"
      "s_cbranch_vccz 2f\n\t"
      "s_sleep %7\n\t"
      "s_cmp_lt_u32 %1, 0x40000\n\t"
      "s_cbranch_scc1 1b\n"
      "2:\n\t"
      "s_mov_b64 exec, %2"
      : "+&v"(v), "+s"(n), "=&s"(sav) : "v"(z), "v"(p), "s"(want), "i"(MASK), "i"(SLEEP) : "memory", "scc", "vcc");
  return v;
}
#define KEEP(x) asm volatile("; keep %0" :: "v"(x))
DI void wait_cnt(unsigned a, unsigned need) {
  unsigned v, n = 0;
  asm volatile(
      "1:\n\t"
      "ds_read_b32 %0, %2\n\t"
      "s_add_u32 %1, %1, 1\n\t"
      "s_waitcnt lgkmcnt(0)\n\t"
      "v_cmp_gt_u32 vcc, %3, %0\n\t"
      "s_cbranch_vccz 2f\n\t"
      "s_sleep 1\n\t"
      "s_cmp_lt_u32 %1, 0x100000\n\t"
      "s_cbranch_scc1 1b\n"
      "2:"
      : "=&v"(v), "+s"(n) : "v"(a), "s"(need) : "memory", "scc", "vcc");
}

#define ST(k)
#define ST_DECL
#define ST_FLUSH()

DI void tile_stats(f32x4 M, f32x4 G, f32x4 km, float* mu, float* rs) {
  float d = fmaf(G[0], km[0], G[1] * km[1]) + fmaf(G[2], km[2], G[3] * km[3]);
  d += dpp<0x124>(d); d += dpp<0x128>(d);
  float q[4];
#pragma unroll
  for (int r = 0; r < 4; ++r) { mu[r] = M[r] * (1.f / C); q[r] = fmaf(-mu[r], mu[r], EPS); }
  q[0] += dpp<0x00>(d); q[1] += dpp<0x55>(d); q[2] += dpp<0xAA>(d); q[3] += dpp<0xFF>(d);
#pragma unroll
  for (int r = 0; r < 4; ++r) rs[r] = __builtin_amdgcn_rsqf(fmx(q[r], EPS));
}

constexpr int hmaj(int j) { return 2 * (j % 3) + j / 3; }
constexpr int ord_unit(int k) { return k < 24 ? (k % 12) / 6 : 4 + (((k - 24) % 4) >> 1); }
constexpr int ord_piece(int k) { return k < 24 ? 3 + 6 * (k / 12) + hmaj(k % 6) : 3 + 2 * ((k - 24) / 4) + (k & 1); }
constexpr int piece_soff(int p) { return ((p - 3) / 6) * (224 * 384) + (((p - 3) % 6) >> 1) * 128 + ((p - 3) & 1) * (8 * 384); }
constexpr bool publish_at(int n) { return n <= 24 ? n % 6 == 0 : n <= 36 ? n % 2 == 0 : false; }
struct LdCtx { u32x4 r0; unsigned voff, o0[4], lb, cnt; };
template <int K> DI void ld_issue(const LdCtx& c) {
  constexpr int U = ord_unit(K), P = ord_piece(K);
  dmai<U * UB + P * 1024, piece_soff(P)>(c.r0, c.voff, c.o0[(U & 1) + (U >> 2) * 2], c.lb);
}
template <int K> DI void ld_steps(const LdCtx& c) {
  if constexpr (K < 36 || K + WIN < 48) {
    if constexpr (K + WIN < 48 || publish_at(K + 1)) waitvm<(K + WIN < 48 ? WIN - 1 : 47 - K)>();
    if constexpr (K + WIN < 48) ld_issue<K + WIN>(c);
    if constexpr (publish_at(K + 1)) *(volatile LAS unsigned*)LP(c.cnt) = K + 1;
    ld_steps<K + 1>(c);
  }
}
template <int K> DI void ld_first(const LdCtx& c) { if constexpr (K < WIN) { ld_issue<K>(c); ld_first<K + 1>(c); } }

__global__ void __launch_bounds__(NTHR, 3) k_all(const u16* f0_, const u16* f1_, const u16* wq_, char* ws_, const u16* q_, const u16* wk_, const u16* wv_,
                                                 const u16* wo_, u16* out) {
  u32x16 ka;
  asm volatile("s_load_dwordx16 %0, %1, 0x0" : "=s"(ka) : "s"(__builtin_amdgcn_kernarg_segment_ptr()));
  u64 pc0; asm volatile("s_getpc_b64 %0" : "=s"(pc0));
  if (threadIdx.x < (CODE_KB * 8 + 63) / 64 * 64) dma4(mkrs((unsigned)pc0 & ~255u, (unsigned)(pc0 >> 32), CODE_KB * 1024), threadIdx.x * 128);
  SB();
  const int b = blockIdx.x, lane = threadIdx.x & 63;
  const int wv = __builtin_amdgcn_readfirstlane(threadIdx.x >> 6);
  const int px = lane >> 3;
  unsigned voff = px * 384 + (((lane & 7) ^ (px & 6)) << 4);
  const bool prod = b >= NPRO && b < NPRO + NPROD;
  constexpr int LQ = 73728, LK = LQ + 6144, LN = LK + 6144, LR = LN + 6144;
  constexpr int LO = 36864, LPR = LO + 73728, LS = LPR + 16 * 208;
  ST_DECL;
  ST(0);
  LdCtx c;
  { const int u0 = (b - NPRO) * 8 + 2 * wv;
    c.lb = wv * (2 * UB); c.cnt = L_SM + 64 + 4 * wv;
#pragma unroll
    for (int k = 0; k < 4; ++k) { const unsigned u = u0 + (k & 1) + (k >> 1) * 4; c.o0[k] = (u * 16 + ((u * 4682) >> 16) * 224) * 384; }
    PINS(c.o0[0]); PINS(c.o0[1]); PINS(c.lb); }
  unsigned g_so, g_lb, g_ss = 128, g_ls = 1024, g_n = 3, g_bytes = C * C * 2, g_sel;
  if (prod) { g_sel = 1; g_bytes = 112 * 112 * 384; g_so = ((b - NPRO) * 8 + wv - 2) * 3072; g_lb = (wv - 2) * UB; g_n = (wv >= 2 && wv < 10) ? 3 : 0; }
  else { g_sel = wv == 0 ? 4 : wv == 1 ? 5 : 2; g_so = wv == 0 ? 0 : wv == 1 ? b * 6144 : (wv - 2) * 128; g_lb = wv < 2 ? LQ + wv * 6144 : (wv - 2) * 24576; g_ss = wv < 2 ? 1024 : 3072;
         g_n = b >= NPRO || wv > 4 ? 0 : wv < 2 ? 6 : 24; if (wv == 0) g_bytes = 16 * C * 2; }
  unsigned pfo = lane < 12 ? (((b >> 3) - 1) * 24 + wv * 12 + lane) * 128 : 0x7fffffffu;
  unsigned gvoff = (!prod && wv < 2) ? lane * 16 : voff;
  PIN(voff); PIN(pfo); PIN(gvoff); PINS(g_so); PINS(g_lb); PINS(g_ss); PINS(g_n); PINS(g_bytes); PINS(g_sel);
  asm volatile("s_waitcnt lgkmcnt(0)" : "+s"(ka));
  c.voff = voff; c.r0 = mkrs(ka[0], ka[1], 224 * 224 * 384);
  if (g_n) {
    const unsigned lo = g_sel == 1 ? ka[2] : g_sel == 2 ? ka[4] : g_sel == 4 ? ka[8] : ka[10], hi = g_sel == 1 ? ka[3] : g_sel == 2 ? ka[5] : g_sel == 4 ? ka[9] : ka[11];
    dma_loop(mkrs(lo, hi, g_bytes), gvoff, g_so, g_ss, g_lb, g_ls, g_n);
  }
  if (prod && wv < 2) {
    dma4(mkrs(ka[4], ka[5], C * C * 2), pfo);
    ld_first<0>(c);
    ST(3);
    *(volatile LAS unsigned*)LP(c.cnt) = 0;
  }
  if (prod) bar();
  int wv1 = wv; PINS(wv1);
  if (prod && wv1 < 2) ld_steps<0>(c);
  char* ws = (char*)(((u64)ka[7] << 32) | ka[6]);
  int wv2 = wv; PINS(wv2);
  if (prod && wv2 < 2) {
    ST(1);
    bar(); bar();
    waitvm<8>(); *(volatile LAS unsigned*)LP(c.cnt) = 40;
    waitvm<4>(); *(volatile LAS unsigned*)LP(c.cnt) = 44;
    waitvm<0>();
    ST(2);
    bar();
    __builtin_amdgcn_endpgm();
  }

  const int i = lane & 15, g = lane >> 4;
  const int tq = i >> 2, tp = i & 3, tpx = 4 * (g & 1) + tq;
  const f32x4 z4 = {0.f, 0.f, 0.f, 0.f};

  if (prod && wv < 10) {
    const int w = b - NPRO, us = wv - 2, v = us & 3;
    unsigned cnta = L_SM + 64 + 4 * (v >> 1);
    unsigned a0 = us * UB + (i >> 3) * 1024 + (i & 7) * 128 + ((g ^ (i & 6)) << 4), a1 = a0 ^ 64;
    unsigned fo0 = us * UB + (i >> 1) * 128 + ((g ^ ((i >> 1) & 6)) << 4), fo1 = fo0 ^ 64;
    unsigned pl = L_P + lane * 16, smi = L_SM + i * 4, smx = L_SM + 128 + i * 32;
    PIN(cnta); PIN(a0); PIN(a1); PIN(fo0); PIN(fo1); PIN(pl); PIN(smi); PIN(smx);
    f32x4 km;
#pragma unroll
    for (int r = 0; r < 4; ++r) km[r] = (i == 4 * g + r) ? 1.f / C : 0.f;
    u32x4 ones = {0x3f803f80u, 0x3f803f80u, 0x3f803f80u, 0x3f803f80u};
    u32x4 sel = (i & 1) ? u32x4{0x3f800000u, 0x3f800000u, 0x3f800000u, 0x3f800000u} : u32x4{0x00003f80u, 0x00003f80u, 0x00003f80u, 0x00003f80u};
    PIN(ones); PIN(km); PIN(sel);
    u32x4 fy[6], f2[6], qk[6];
    f32x4 M1 = z4, G1 = z4;
    float nsq;
    waitvm<0>();
#pragma unroll
    for (int s = 0; s < 6; ++s) fy[s] = R128((s & 1) ? fo1 : fo0, (s >> 1) * 1024);
    SB();
#pragma unroll
    for (int s = 0; s < 6; ++s) {
      M1 = MFMA(fy[s], ones, M1); G1 = MFMA(fy[s], fy[s], G1);
#pragma unroll
      for (int j = 0; j < 4; ++j) { const unsigned x = fy[s][j]; f2[s][j] = __builtin_bit_cast(unsigned, __builtin_bit_cast(u16x2, x) + u16x2{0x80, 0x80}); }
    }
#pragma unroll
    for (int s = 0; s < 6; ++s) PIN(f2[s]);
    PIN(M1); PIN(G1);
    auto mg2 = [&](const u32x4& t0, const u32x4& t1, int c3, f32x4& M, f32x4& G) {
      G = MFMA(t0, t0, G); M = MFMA(t0, ones, M); G = MFMA(t0, f2[2 * c3], G);
      M = MFMA(t1, ones, M); G = MFMA(t1, t1, G); G = MFMA(t1, f2[2 * c3 + 1], G);
    };
    auto l_bad = [&](float L) { return FORCE_SLOW || __any(!(L >= 8.2718061e-25f && L <= 1.2089258e24f)); };
    if (wv < 6) {
      u32x4 ta[6], tb[6];
      float mu[8], rs[8];
      unsigned tro[3], trh[3], trf[3];
#pragma unroll
      for (int cc = 0; cc < 3; ++cc) {
        const int cb = 3 * v + cc, c3 = cb >> 2, a = 2 * (cb & 3);
        tro[cc] = c3 * 2048 + (g >> 1) * 1024 + tpx * 128 + (((a + (tp >> 1)) ^ (tpx & 6)) << 4) + 8 * (tp & 1);
        trh[cc] = tro[cc] + 4 * UB;
        const int fpx = 2 * g + (tq & 1);
        trf[cc] = 2 * UB + (tq >> 1) * UB + c3 * 1024 + fpx * 128 + (((a + (tp >> 1)) ^ (fpx & 6)) << 4) + 8 * (tp & 1);
        PIN(tro[cc]); PIN(trh[cc]); PIN(trf[cc]);
      }
      int rec = WS_SLAB + (i * 2 + (v >> 1)) * SLABR + w * REC + g * 16;
      unsigned pw = pl + v * 1024, pw2 = pl + 6 * 1024 + v * 4;
      PIN(rec); PIN(pw); PIN(pw2);
      auto tile6 = [&](int base, u32x4* T, float* mu_, float* rs_) {
#pragma unroll
        for (int s = 0; s < 6; ++s) T[s] = R128((s & 1) ? a1 : a0, (base + (s & ~1)) * 1024);
        SB();
        f32x4 M = M1, G = G1;
#pragma unroll
        for (int c3 = 0; c3 < 3; ++c3) mg2(T[2 * c3], T[2 * c3 + 1], c3, M, G);
        tile_stats(M, G, km, mu_, rs_);
#pragma unroll
        for (int r = 0; r < 4; ++r) { PIN(mu_[r]); PIN(rs_[r]); }
      };
      wait_cnt(cnta, 6 + 6 * (v & 1));
      tile6(3, ta, mu, rs);
      wait_cnt(cnta, 18 + 6 * (v & 1));
      tile6(9, tb, mu + 4, rs + 4);
      ST(1);
      bar();
      ST(2);
      nsq = R32F(smi, 0);
#pragma unroll
      for (int s = 0; s < 6; ++s) qk[s] = R128(pl, O_QK + s * 1024);
      SB();
      f32x4 S1 = z4, Sa, Sb;
#pragma unroll
      for (int r = 0; r < 4; ++r) { Sa[r] = mu[r] * nsq; Sb[r] = mu[4 + r] * nsq; }
#pragma unroll
      for (int s = 0; s < 6; ++s) { S1 = MFMA(fy[s], qk[s], S1); Sa = MFMA(ta[s], qk[s], Sa); }
#pragma unroll
      for (int s = 0; s < 6; ++s) Sb = MFMA(tb[s], qk[s], Sb);
      float s_[8], ref = 0.f;
#pragma unroll
      for (int r = 0; r < 4; ++r) { s_[r] = (Sa[r] + S1[r]) * rs[r]; s_[4 + r] = (Sb[r] + S1[r]) * rs[4 + r]; }
      auto emit = [&]() {
        float l = 0.f, t = 0.f, p[8];
#pragma unroll
        for (int r = 0; r < 8; ++r) { const float e = __builtin_amdgcn_exp2f(s_[r]); l += e; p[r] = e * rs[r]; t = fmaf(p[r], mu[r], t); }
        W128(pw, 0, u32x4{cvtpk(p[0], p[1]), cvtpk(p[2], p[3]), cvtpk(p[4], p[5]), cvtpk(p[6], p[7])});
        W32(pw2, 0, cvtpk((p[0] + p[1]) + (p[4] + p[5]), (p[2] + p[3]) + (p[6] + p[7])));
        W32(pw2, 1024, cvtpk(l, t));
      };
      emit();
      u32x4 xt[21];
      auto ldxa = [&]() {
#pragma unroll
        for (int k = 0; k < 12; ++k) xt[k] = TR2(tro[k % 3], (k / 3) * UB + 3072, tro[k % 3], (k / 3) * UB + 9216);
#pragma unroll
        for (int k = 0; k < 6; ++k) xt[12 + k] = TR2(trh[k % 3], (k / 3) * 2 * UB + 3072, trh[k % 3], (k / 3) * 2 * UB + UB + 3072);
#pragma unroll
        for (int k = 0; k < 3; ++k) xt[18 + k] = TR2(trf[k] - 2 * UB, 0, trf[k], 0);
      };
      SB();
      asm volatile("s_waitcnt lgkmcnt(0)" ::: "memory");
      ldxa();
      SB();
      asm volatile("s_barrier" ::: "memory");
      SB();
      ST(3);
      f32x4 acc[3], LT;
      auto pva = [&]() {
        u32x4 pf[8];
#pragma unroll
        for (int k = 0; k < 8; ++k) pf[k] = R128(pl, k * 1024);
        SB();
#pragma unroll
        for (int k = 0; k < 3; ++k) acc[k] = z4;
#pragma unroll
        for (int k = 0; k < 21; ++k) acc[k % 3] = MFMA(xt[k], pf[k / 3], acc[k % 3]);
        LT = MFMA(sel, pf[7], z4);
      };
      pva();
      auto ldxf = [&]() {
#pragma unroll
        for (int k = 0; k < 3; ++k) xt[6 + k] = TR2(trf[k], 2 * UB, trf[k], 4 * UB);
      };
      ldxf();
#pragma unroll
      for (int k = 0; k < 3; ++k) { PIN(xt[6 + k]); PIN(acc[k]); }
      PIN(LT);
      ST(4);
      bar();
      auto ldxb = [&]() {
#pragma unroll
        for (int k = 0; k < 6; ++k) xt[k] = TR2(trh[k % 3], (k / 3) * 2 * UB + 9216, trh[k % 3], (k / 3) * 2 * UB + UB + 9216);
      };
      ldxb();
#pragma unroll
      for (int k = 0; k < 6; ++k) PIN(xt[k]);
      bar();
      ST(5);
      u32x4 pf[4]; f32x4 lt;
      auto rd = [&]() {
        pf[3] = R128(pl, 11 * 1024);
#pragma unroll
        for (int k = 0; k < 3; ++k) pf[k] = R128(pl, (8 + k) * 1024);
        SB();
        lt = MFMA(sel, pf[3], LT);
#pragma unroll
        for (int k = 0; k < 3; ++k) acc[k] = MFMA(xt[k], pf[0], acc[k]);
      };
      rd();
      if (__builtin_expect(l_bad(lt[0]), 0)) {
        W32(smx + us * 4, 0, __float_as_uint(xrow_max(fmx(fmx(fmx(s_[0], s_[1]), fmx(s_[2], s_[3])), fmx(fmx(s_[4], s_[5]), fmx(s_[6], s_[7]))))));
        bar();
        ref = fmx(fmx4(R128F(smx, 0)), fmx4(R128F(smx, 16)));
#pragma unroll
        for (int r = 0; r < 8; ++r) s_[r] -= ref;
        emit();
        bar();
        ldxa(); pva(); ldxf(); ldxb(); rd();
      }
#pragma unroll
      for (int k = 0; k < 3; ++k) acc[k] = MFMA(xt[3 + k], pf[1], acc[k]);
#pragma unroll
      for (int k = 0; k < 3; ++k) acc[k] = MFMA(xt[6 + k], pf[2], acc[k]);
      const float il = __builtin_amdgcn_rcpf(lt[0]), nt = -lt[1] * il;
      const __amdgpu_buffer_rsrc_t rw = __builtin_amdgcn_make_buffer_rsrc(ws, 0, WS_BYTES, 0x00020000);
      if (!(v & 1) && g == 0) __builtin_amdgcn_raw_buffer_store_b32(__float_as_uint(ref + __builtin_amdgcn_logf(lt[0])), rw, rec + 192, 0, 16);
      unsigned o[6];
#pragma unroll
      for (int k = 0; k < 3; ++k) {
        o[2 * k] = cvtpk_h(fmaf(acc[k][0], il, nt), fmaf(acc[k][1], il, nt));
        o[2 * k + 1] = cvtpk_h(fmaf(acc[k][2], il, nt), fmaf(acc[k][3], il, nt));
      }
      __builtin_amdgcn_raw_buffer_store_b128(u32x4{o[0], o[1], o[2], o[3]}, rw, rec, (v & 1) * 64, 16);
      __builtin_amdgcn_raw_buffer_store_b64(u32x2{o[4], o[5]}, rw, rec + 128, (v & 1) * 8, 16);
      ST(6);
      ST_FLUSH();
      __builtin_amdgcn_endpgm();
    } else {
      u32x4 ta[6];
      float mu[8], rs[8];
      unsigned pwa = pl + (4 + (v >> 1)) * 1024 + (v & 1) * 8, pw2 = pl + 10 * 1024 + v * 4;
      GAS unsigned* sigp = (GAS unsigned*)(ws + WS_CNT) + (i * 28 + w / 7) * 32;
      PIN(pwa); PIN(pw2); PIN(sigp);
      { f32x4 M = M1, G = G1;
#pragma unroll
        for (int c3 = 0; c3 < 3; ++c3) {
          wait_cnt(cnta, 26 + 4 * c3 + 2 * (v & 1));
          ta[2 * c3] = R128(a0, (3 + 2 * c3) * 1024); ta[2 * c3 + 1] = R128(a1, (3 + 2 * c3) * 1024);
          SB();
          mg2(ta[2 * c3], ta[2 * c3 + 1], c3, M, G);
          PIN(M); PIN(G);
        }
        tile_stats(M, G, km, mu, rs); }
#pragma unroll
      for (int r = 0; r < 4; ++r) { PIN(mu[r]); PIN(rs[r]); }
      ST(1);
      bar();
      ST(2);
      nsq = R32F(smi, 0);
#pragma unroll
      for (int s = 0; s < 6; ++s) qk[s] = R128(pl, O_QK + s * 1024);
      SB();
      f32x4 Sd = z4, Sa;
#pragma unroll
      for (int r = 0; r < 4; ++r) Sa[r] = mu[r] * nsq;
#pragma unroll
      for (int s = 0; s < 6; ++s) { Sd = MFMA(fy[s], qk[s], Sd); Sa = MFMA(ta[s], qk[s], Sa); }
      float s_[8], lA, tA, pf0, pf1;
#pragma unroll
      for (int r = 0; r < 4; ++r) s_[r] = (Sa[r] + Sd[r]) * rs[r];
      auto emitA = [&]() {
        float e[4], p[4];
#pragma unroll
        for (int r = 0; r < 4; ++r) { e[r] = __builtin_amdgcn_exp2f(s_[r]); p[r] = e[r] * rs[r]; }
        W64(pwa, 0, u32x2{cvtpk(p[0], p[1]), cvtpk(p[2], p[3])});
        lA = (e[0] + e[1]) + (e[2] + e[3]); tA = fmaf(p[0], mu[0], p[1] * mu[1]) + fmaf(p[2], mu[2], p[3] * mu[3]);
        pf0 = p[0] + p[1]; pf1 = p[2] + p[3];
      };
      emitA();
      bar();
      ST(3);
      f32x4 LT = MFMA(sel, R128(pl, 7 * 1024), z4);
      PIN(lA); PIN(tA); PIN(pf0); PIN(pf1);
      auto bstep = [&](int c3) {
        const u32x4 t0 = R128(a0, (9 + 2 * c3) * 1024), t1 = R128(a1, (9 + 2 * c3) * 1024);
        SB();
        mg2(t0, t1, c3, M1, G1);
        Sd = MFMA(t0, qk[2 * c3], Sd); Sd = MFMA(t1, qk[2 * c3 + 1], Sd);
      };
      wait_cnt(cnta, 40); bstep(0);
      wait_cnt(cnta, 44); bstep(1);
      PIN(M1); PIN(G1); PIN(Sd); PIN(LT);
      bar();
      ST(4);
      bstep(2);
      tile_stats(M1, G1, km, mu + 4, rs + 4);
#pragma unroll
      for (int r = 0; r < 4; ++r) s_[4 + r] = fmaf(mu[4 + r], nsq, Sd[r]) * rs[4 + r];
      auto emitB = [&]() {
        float e[4], p[4];
#pragma unroll
        for (int r = 0; r < 4; ++r) { e[r] = __builtin_amdgcn_exp2f(s_[4 + r]); p[r] = e[r] * rs[4 + r]; }
        W64(pwa, 4096, u32x2{cvtpk(p[0], p[1]), cvtpk(p[2], p[3])});
        W32(pw2, 0, cvtpk(pf0 + (p[0] + p[1]), pf1 + (p[2] + p[3])));
        W32(pw2, 1024, cvtpk((lA + e[0]) + (e[1] + (e[2] + e[3])), fmaf(p[0], mu[4], fmaf(p[1], mu[5], tA)) + fmaf(p[2], mu[6], p[3] * mu[7])));
      };
      emitB();
      bar();
      ST(5);
      const f32x4 lt = MFMA(sel, R128(pl, 11 * 1024), LT);
      if (__builtin_expect(l_bad(lt[0]), 0)) {
        W32(smx + us * 4, 0, __float_as_uint(xrow_max(fmx(fmx(fmx(s_[0], s_[1]), fmx(s_[2], s_[3])), fmx(fmx(s_[4], s_[5]), fmx(s_[6], s_[7]))))));
        bar();
        const float ref = fmx(fmx4(R128F(smx, 0)), fmx4(R128F(smx, 16)));
#pragma unroll
        for (int r = 0; r < 8; ++r) s_[r] -= ref;
        emitA(); emitB();
        bar();
      }
      if (v == 0 && lane < 16) __hip_atomic_fetch_add(sigp, 1u, RLX);
      ST(6);
      ST_FLUSH();
      __builtin_amdgcn_endpgm();
    }
  }

  if (prod) {
    const int w = b - NPRO;
    const unsigned ep = __hip_atomic_load((GAS unsigned*)(ws + WS_EPOCH), RLX);
    { const unsigned po = lane < 24 ? (((b >> 3) - 1) * 24 + lane) * 128 : 0x7fffffffu;
      dma4(mkrs(ka[14], ka[15], C * C * 2), po); dma4(mkrs(ka[12], ka[13], C * C * 2), po); }
    const unsigned tag = __builtin_amdgcn_readfirstlane(ep) + 1u;
    const __amdgpu_buffer_rsrc_t rw = __builtin_amdgcn_make_buffer_rsrc(ws, 0, WS_BYTES, 0x00020000);
    GAS u64* rp = (GAS u64*)(ws + WS_RDY) + lane;
    const u64 tag2 = ((u64)tag << 32) | tag;
    unsigned kone = 0xbf80bf80u, go = WS_QK + lane * 16, qw = L_P + O_QK + lane * 16, smi = L_SM + i * 4, tagv = tag;
    PIN(rp); PIN(kone); PIN(go); PIN(qw); PIN(smi); PIN(tagv);
    const unsigned pv = poll_atomic<0xfffu, 2>((GAS unsigned*)(ws + WS_QF) + (w & 31) * 32 + (lane < 12 ? lane : 0), tag);
    ST(1);
    u64 rdy; float sq; int tries = 0;
    for (int sp = 0; sp < SPIN; ++sp) {
      asm volatile("" ::: "memory");
      u32x4 x[12];
#pragma unroll
      for (int k = 0; k < 12; ++k) x[k] = __builtin_amdgcn_raw_buffer_load_b128(rw, go, k * 1024, 16);
      rdy = __hip_atomic_load(rp, RLX);
      SB();
      unsigned bad = 0; float s0 = 0.f;
#pragma unroll
      for (int k = 0; k < 12; ++k) {
        asm volatile("ds_write2_b32 %0, %1, %2 offset0:%3 offset1:%4" :: "v"(qw), "v"(x[k][0]), "v"(x[k][2]), "i"((k & 1) * 2), "i"((k & 1) * 2 + 1) : "memory");
        if (k & 1) qw += 1024;
        bad |= (x[k][1] ^ tagv); bad |= (x[k][3] ^ tagv);
        s0 = dot2(x[k][0], kone, s0); s0 = dot2(x[k][2], kone, s0);
        SB();
      }
      qw -= 6144;
      sq = s0;
      ++tries;
      if (!__any(bad != 0)) break;
    }
    KEEP(pv);
    W32(smi, 0, __float_as_uint(xrow_sum(sq)));
    ST(2);
    bar();
    for (int sp = 0; sp < SPIN * 16 && __any(rdy != tag2); ++sp) rdy = __hip_atomic_load(rp, RLX);
    __builtin_amdgcn_endpgm();
  }

  if (b < NPRO) {
    if (wv == 0) {
      unsigned qa = LQ + i * 384 + g * 16, wq_a = LN + lane * 16, klo = 0x00003f80u, khi = 0x3f800000u;
      u32x4 ones = {0x3f803f80u, 0x3f803f80u, 0x3f803f80u, 0x3f803f80u};
      f32x4 km;
#pragma unroll
      for (int r = 0; r < 4; ++r) km[r] = (i == 4 * g + r) ? 1.f / C : 0.f;
      PIN(qa); PIN(wq_a); PIN(klo); PIN(khi); PIN(ones); PIN(km);
      waitvm<0>();
      ST(1);
      u32x4 qf[6];
#pragma unroll
      for (int s = 0; s < 6; ++s) qf[s] = R128(qa, s * 64);
      SB();
      f32x4 M = z4, G = z4;
#pragma unroll
      for (int s = 0; s < 6; ++s) M = MFMA(ones, qf[s], M);
      const float nmean = M[0] * (-1.f / C);
#pragma unroll
      for (int s = 0; s < 6; ++s) {
        u32x4 qc, t;
        asm("v_dot2_f32_bf16 %0, %8, %12, %14\n\tv_dot2_f32_bf16 %4, %8, %13, %14\n\tv_dot2_f32_bf16 %1, %9, %12, %14\n\tv_dot2_f32_bf16 %5, %9, %13, %14\n\t"
            "v_dot2_f32_bf16 %2, %10, %12, %14\n\tv_dot2_f32_bf16 %6, %10, %13, %14\n\tv_dot2_f32_bf16 %3, %11, %12, %14\n\tv_dot2_f32_bf16 %7, %11, %13, %14\n\t"
            "v_cvt_pk_bf16_f32 %0, %0, %4\n\tv_cvt_pk_bf16_f32 %1, %1, %5\n\tv_cvt_pk_bf16_f32 %2, %2, %6\n\tv_cvt_pk_bf16_f32 %3, %3, %7"
            : "=&v"(qc[0]), "=&v"(qc[1]), "=&v"(qc[2]), "=&v"(qc[3]), "=&v"(t[0]), "=&v"(t[1]), "=&v"(t[2]), "=&v"(t[3])
            : "v"(qf[s][0]), "v"(qf[s][1]), "v"(qf[s][2]), "v"(qf[s][3]), "v"(klo), "v"(khi), "v"(nmean));
        W128(wq_a, s * 1024, qc);
      }
      SB();
#pragma unroll
      for (int s = 0; s < 6; ++s) G = MFMA(qf[s], qf[s], G);
      constexpr float SC = 0.07216878364870322f * 1.4426950408889634f;
      ST(2);
      bar();
      const float d = xrow_sum(fmaf(G[0], km[0], G[1] * km[1]) + fmaf(G[2], km[2], G[3] * km[3]));
      W32(LR + 64 + i * 4, 0, __float_as_uint(SC * __builtin_amdgcn_rsqf(fmx(d + fmaf(-nmean, nmean, EPS), EPS))));
      bar();
    } else if (wv == 1) {
      const unsigned tag = __hip_atomic_load((GAS unsigned*)(ws + WS_EPOCH), RLX) + 1u;
      W32(LR, 0, tag);
      waitvm<0>();
      ST(1);
      bar();
    } else if (wv < 5) {
      const int j3 = wv - 2;
      unsigned o0[4], o1[4];
#pragma unroll
      for (int db = 0; db < 4; ++db) {
        o0[db] = j3 * 24576 + g * 1024 + tq * 128 + 8 * (tp & 1) + (((2 * db + (tp >> 1)) ^ (tq & 6)) << 4);
        o1[db] = j3 * 24576 + g * 1024 + tq * 128 + 8 * (tp & 1) + 512 + (((2 * db + (tp >> 1)) ^ ((4 + tq) & 6)) << 4);
        PIN(o0[db]); PIN(o1[db]);
      }
      unsigned ka0 = LK + i * 384 + j3 * 128 + g * 8, rl = LR + lane * 16, rwz = rl + j3 * 1024, qn = LN + lane * 16, ra = LR + 64 + i * 4;
      int dsto = WS_QK + ((b >> 1) * 2 + (g & 1)) * 1024 + (((b & 1) * 2 + (g >> 1)) * 16 + i) * 16;
      GAS unsigned* fp = (GAS unsigned*)(ws + WS_QF) + (lane & 31) * 32 + b;
      GAS u64* dst = (GAS u64*)(ws + dsto);
      ST(4);
      PIN(ka0); PIN(rl); PIN(rwz); PIN(qn); PIN(ra); PIN(fp); PIN(dst);
      bar();
      ST(1);
      u32x4 qc[6], kf[2];
#pragma unroll
      for (int s = 0; s < 6; ++s) qc[s] = R128(qn, s * 1024);
#pragma unroll
      for (int kk = 0; kk < 2; ++kk) { const u32x2 lo = R64(ka0, kk * 64), hi = R64(ka0, kk * 64 + 32); kf[kk] = u32x4{lo[0], lo[1], hi[0], hi[1]}; }
      unsigned tag = __float_as_uint(R32F(LR, 0));
      f32x4 Q[4] = {z4, z4, z4, z4};
      auto kstep = [&](int ks) {
        u32x4 wf[4];
#pragma unroll
        for (int db = 0; db < 4; ++db) wf[db] = TR2(o0[db], ks * 4096, o1[db], ks * 4096);
        SB();
#pragma unroll
        for (int db = 0; db < 4; ++db) Q[db] = MFMA(wf[db], qc[ks], Q[db]);
      };
      waitvm<20>(); kstep(0); waitvm<16>(); kstep(1); waitvm<12>(); kstep(2); waitvm<8>(); kstep(3);
      PIN(tag); PIN(kf[0]); PIN(kf[1]);
      if (PRO_SIG >= 4) { waitvm<PRO_SIG>(); if (wv == 4) asm volatile("global_atomic_swap %0, %1, off" :: "v"(fp), "v"(tag) : "memory"); else asm volatile("s_nop 0"); }
      if (wv == 4 && PRO_SIG >= 4) waitvm<5>(); else waitvm<4>();
      kstep(4);
#pragma unroll
      for (int db = 0; db < 4; ++db) PIN(Q[db]);
      if (PRO_SIG < 4 && wv == 4) { waitvm<PRO_SIG>(); asm volatile("global_atomic_swap %0, %1, off" :: "v"(fp), "v"(tag) : "memory"); }
      if (wv == 4) waitvm<1>(); else waitvm<0>();
      ST(2);
      kstep(5);
      f32x4 R = z4;
#pragma unroll
      for (int kk = 0; kk < 2; ++kk)
        R = MFMA(kf[kk], u32x4{cvtpk(Q[2 * kk][0], Q[2 * kk][1]), cvtpk(Q[2 * kk][2], Q[2 * kk][3]), cvtpk(Q[2 * kk + 1][0], Q[2 * kk + 1][1]), cvtpk(Q[2 * kk + 1][2], Q[2 * kk + 1][3])}, R);
      if (j3 != 0) W128(rwz, 0, __builtin_bit_cast(u32x4, R));
      bar();
      if (j3 == 0) {
        const f32x4 r1 = R128F(rl, 1024), r2 = R128F(rl, 2048);
        const float rstd = R32F(ra, 0);
        R = (R + r1 + r2) * rstd;
        __hip_atomic_store(dst, ((u64)tag << 32) | cvtpk(R[0], R[1]), RLX);
        __hip_atomic_store(dst + 1, ((u64)tag << 32) | cvtpk(R[2], R[3]), RLX);
        ST(3);
      }
    }
    if (wv < 5) { ST_FLUSH(); }
    __builtin_amdgcn_endpgm();
  }

  {
    if (wv >= 8) __builtin_amdgcn_endpgm();
    const int rb = b - NPRO - NPROD, n = rb >> 1, h = rb & 1, x = wv & 3;
    const __amdgpu_buffer_rsrc_t rw = __builtin_amdgcn_make_buffer_rsrc(ws, 0, WS_BYTES, 0x00020000);
    const int slab = WS_SLAB + rb * SLABR;
    if (wv < 4) {
      const unsigned ep = __hip_atomic_load((GAS unsigned*)(ws + WS_EPOCH), RLX);
      const u32x4 sv = {SENT, SENT, SENT, SENT};
#pragma unroll
      for (int k = 0; k < 10; ++k) __builtin_amdgcn_raw_buffer_store_b128(sv, rw, slab + x * 10240 + lane * 16, k * 1024, 16);
      if (x == 0) {
        if (h == 0) { if (lane < 28) __hip_atomic_store((GAS unsigned*)(ws + WS_CNT) + (n * 28 + lane) * 32, 0u, RLX); }
        else if (lane < 48) __hip_atomic_store((GAS u64*)(out + n * C) + lane, 0ull, RLX);
      }
      GAS unsigned* fp = (GAS unsigned*)(ws + WS_RDY) + rb * 4 + x;
      unsigned tag = ep + 1u;
      W32(LS, 64, tag);
      waitvm<0>();
      if (lane == 0) asm volatile("global_atomic_swap %0, %1, off" :: "v"(fp), "v"(tag) : "memory");
      ST(1);
      asm volatile("s_waitcnt lgkmcnt(0)" ::: "memory");
      __builtin_amdgcn_endpgm();
    }
    { const u32x4 rv = mkrs(ka[12], ka[13], C * C * 2), ro = mkrs(ka[14], ka[15], C * C * 2);
#pragma unroll
      for (int c3 = 0; c3 < 3; ++c3) dma_loop(ro, voff, x * (48 * 384) + c3 * 128, 3072, LO + c3 * 24576 + x * 6144, 1024, 6);
#pragma unroll
      for (int c3 = 0; c3 < 3; ++c3) dma_loop(rv, voff, h * (96 * 384) + x * (24 * 384) + c3 * 128, 3072, c3 * 12288 + x * 3072, 1024, 3); }
    float qv = 0.f;
    if (h == 0 && g < 3) qv = bf_lo(((GAS const u16*)(((u64)ka[9] << 32) | ka[8]))[n * C + 48 * x + 16 * g + i]);
    unsigned b0[3], b1[3];
#pragma unroll
    for (int e = 0; e < 3; ++e) {
      const int cb = 3 * x + e, c3 = cb >> 2, a = 2 * (cb & 3);
      b0[e] = LO + c3 * 24576 + g * 1024 + tq * 128 + 8 * (tp & 1) + (((a + (tp >> 1)) ^ (tq & 6)) << 4);
      b1[e] = LO + c3 * 24576 + g * 1024 + tq * 128 + 8 * (tp & 1) + 512 + (((a + (tp >> 1)) ^ ((4 + tq) & 6)) << 4);
      PIN(b0[e]); PIN(b1[e]);
    }
    unsigned a0 = (i >> 3) * 1024 + (i & 7) * 128 + ((g ^ (i & 6)) << 4), a1 = a0 ^ 64;
    PIN(a0); PIN(a1);
    waitvm<0>();
    PIN(qv);
    bar();
    ST(2);
    u32x4 W[3][3];
    {
      u32x4 Bo[3][6], af[6];
      auto lda = [&](int t, int s) { const int ks = t >> 1, hb = t & 1, cb = ks < 2 ? 3 * ks + hb : 2 + 3 * hb; return R128((s & 1) ? a1 : a0, (s >> 1) * 12288 + cb * 2048); };
#pragma unroll
      for (int ks = 0; ks < 6; ++ks) {
        af[ks] = lda(0, ks);
#pragma unroll
        for (int e = 0; e < 3; ++e) Bo[e][ks] = TR2(b0[e], ks * 4096, b1[e], ks * 4096);
      }
      SB();
      f32x4 D[2][3];
#pragma unroll
      for (int t = 0; t < 6; ++t) {
#pragma unroll
        for (int s = 0; s < 6; ++s) {
#pragma unroll
          for (int e = 0; e < 3; ++e) D[t & 1][e] = MFMA(af[s], Bo[e][s], s ? D[t & 1][e] : z4);
          if (t < 5) af[s] = lda(t + 1, s);
          if (s == 1 && t > 0) {
#pragma unroll
            for (int e = 0; e < 3; ++e) { const f32x4 dd = D[(t - 1) & 1][e]; W[e][(t - 1) >> 1][2 * ((t - 1) & 1)] = cvtpk(dd[0], dd[1]); W[e][(t - 1) >> 1][2 * ((t - 1) & 1) + 1] = cvtpk(dd[2], dd[3]); }
          }
          SB();
        }
      }
#pragma unroll
      for (int e = 0; e < 3; ++e) { W[e][2][2] = cvtpk(D[1][e][0], D[1][e][1]); W[e][2][3] = cvtpk(D[1][e][2], D[1][e][3]); }
#pragma unroll
      for (int e = 0; e < 3; ++e) { PIN(W[e][0]); PIN(W[e][1]); PIN(W[e][2]); }
    }
    ST(3);
    unsigned aro = LPR + (i & 7) * 208 + g * 16, mro = LS + 16 * (g & 1);
    GAS s16x2* op = (GAS s16x2*)(out + n * C + 48 * x + 16 * (g < 3 ? g : 0) + i);
    int doff = slab + (49 * x + g) * REC + (i < 12 ? i : 11) * 16, dlast = slab + (49 * x + 48) * REC + (i < 12 ? i : 11) * 16;
    const bool lval = 4 * i + g < 49;
    int loff = slab + (49 * x + (lval ? 4 * i + g : 0)) * REC + 192;
    float lmask = lval ? 0.f : -INFINITY;
    unsigned woff = LPR + (2 * x + (g & 1)) * 208 + (i < 12 ? i : 12) * 16 + (g >> 1) * 8, mwo = LS + 8 * x;
    PIN(aro); PIN(mro); PIN(op); PIN(doff); PIN(dlast); PIN(loff); PIN(lmask); PIN(woff); PIN(mwo);
    const unsigned pv = poll_atomic<0x7fu, 2>((GAS unsigned*)(ws + WS_CNT) + (n * 28 + 7 * x + (lane < 7 ? lane : 0)) * 32, 7u);
    ST(4);
    int tries = 0;
    u32x2 pk;
    for (int sp = 0; sp < SPIN; ++sp) {
      asm volatile("" ::: "memory");
      u32x4 d[13];
      const unsigned lse = __builtin_amdgcn_raw_buffer_load_b32(rw, loff, 0, 16);
#pragma unroll
      for (int k = 0; k < 12; ++k) d[k] = __builtin_amdgcn_raw_buffer_load_b128(rw, doff, k * (4 * REC), 16);
      d[12] = __builtin_amdgcn_raw_buffer_load_b128(rw, dlast, 0, 16);
      SB();
      const float ls = __uint_as_float(lse) + lmask;
      const float Ml = xrow_max(row16_max(ls));
      const float gw = __builtin_amdgcn_exp2f(ls - Ml);
      float ac[8];
#pragma unroll
      for (int j = 0; j < 8; ++j) ac[j] = 0.f;
      SB();
#define GSTEP(k) { const float wgt = dpp<0x150 + k>(gw); \
        _Pragma("unroll") for (int j = 0; j < 4; ++j) { const unsigned dj = d[k][j]; \
          asm("v_fma_mix_f32 %0, %1, %2, %0 op_sel_hi:[1,0,0]" : "+v"(ac[2 * j]) : "v"(dj), "v"(wgt)); \
          asm("v_fma_mix_f32 %0, %1, %2, %0 op_sel:[1,0,0] op_sel_hi:[1,0,0]" : "+v"(ac[2 * j + 1]) : "v"(dj), "v"(wgt)); } SB(); }
      GSTEP(0) GSTEP(1)
      { const float Ll = xrow_sum(row16_sum(gw)); W64(mwo, 0, u32x2{__float_as_uint(Ml), __float_as_uint(Ll)}); SB(); }
      GSTEP(2) GSTEP(3) GSTEP(4) GSTEP(5) GSTEP(6) GSTEP(7) GSTEP(8) GSTEP(9) GSTEP(10) GSTEP(11) GSTEP(12)
      const float f0 = fold32(ac[0], ac[4]), f1 = fold32(ac[1], ac[5]), f2 = fold32(ac[2], ac[6]), f3 = fold32(ac[3], ac[7]);
      pk = u32x2{cvtpk(f0, f1), cvtpk(f2, f3)};
      W64(woff, 0, pk);
      ++tries;
      if (!__any(__builtin_isunordered(f0, f2))) break;
    }
    KEEP(pv);
    ST(5);
    bar();
    {
      u32x4 af[3];
#pragma unroll
      for (int ks = 0; ks < 3; ++ks) af[ks] = R128(aro, ks * 64);
      const f32x4 mine = R128F(mro, 0);
      SB();
      f32x4 D[3] = {z4, z4, z4};
#pragma unroll
      for (int ks = 0; ks < 3; ++ks)
#pragma unroll
        for (int e = 0; e < 3; ++e) D[e] = MFMA(af[ks], W[e][ks], D[e]);
      float M = fmx(mine[0], mine[2]);
      { auto s = __builtin_amdgcn_permlane16_swap(__float_as_uint(M), __float_as_uint(M), false, false); M = fmx(__uint_as_float(s[0]), __uint_as_float(s[1])); }
      float fa = __builtin_amdgcn_exp2f(mine[0] - M), fb = __builtin_amdgcn_exp2f(mine[2] - M);
      const float Lh = fmaf(fa, mine[1], fb * mine[3]);
      const float il = __builtin_amdgcn_rcpf(fold16(Lh, Lh));
      fa *= il; fb *= il;
      float y[3];
#pragma unroll
      for (int e = 0; e < 3; ++e) y[e] = (D[e][0] + D[e][1]) * fa + (D[e][2] + D[e][3]) * fb;
      const float ya = g < 2 ? y[0] : y[2], yb = g < 2 ? y[1] : y[2];
      const float ys = fold16(ya, yb) + qv;
      const unsigned o = cvtpk(ys, dpp<0xB1>(ys));
      if (g < 3 && !(i & 1)) __builtin_amdgcn_global_atomic_fadd_v2bf16(op, __builtin_bit_cast(s16x2, o));
      if (rb == 0 && wv == 4 && lane == 0) __hip_atomic_store((GAS unsigned*)(ws + WS_EPOCH), __float_as_uint(R32F(LS, 64)), RLX);
    }
    ST(6);
    ST_FLUSH();
  }
}

extern "C" void kernel_launch(void* const* d_in, const int* in_sizes, int n_in, void* d_out, int out_size, void* d_ws,
                              size_t ws_size, hipStream_t stream) {
  (void)hipFuncSetAttribute((const void*)k_all, hipFuncAttributeMaxDynamicSharedMemorySize, LDS_BYTES);
  k_all<<<NBLK, NTHR, LDS_BYTES, stream>>>((const u16*)d_in[1], (const u16*)d_in[2], (const u16*)d_in[3], (char*)d_ws + WS_OFF(ws_size), (const u16*)d_in[0],
                                           (const u16*)d_in[4], (const u16*)d_in[5], (const u16*)d_in[6], (u16*)d_out);
}
```
